```python
import torch

def _softmax(x, axis=-1):
    return torch.softmax(x, dim=axis)
def _gelu(x):
    return torch.nn.functional.gelu(x, approximate="tanh")
def _silu(x):
    return torch.nn.functional.silu(x)
def _sigmoid(x):
    return torch.sigmoid(x)
def _layernorm(x, axis=-1, eps=1e-5):
    m = x.mean(dim=axis, keepdim=True)
    v = x.var(dim=axis, keepdim=True, unbiased=False)
    return (x - m) / torch.sqrt(v + eps)
def _scatter_add(tgt, idx, src, axis=0):
    return tgt.clone().index_add_(axis, idx, src)
def _segment_sum(src, seg, n):
    out = torch.zeros((n,) + src.shape[1:], dtype=src.dtype)
    return out.index_add_(0, seg, src)
def _pad(x, pw):
    flat = tuple(v for pair in reversed(pw) for v in pair)
    return torch.nn.functional.pad(x, flat)

kernel_name = 'sequential_preLN__mha_block_cached__swiglu_ffn__decode'

def setup_inputs():

    _w = (lambda a: (lambda t: t.to(torch.bfloat16) if t.is_floating_point() else t)(torch.as_tensor(a)))
    def _A_setup():
        B, Sq, D = 1, 1, 512
        H, Sk, Dh = 32, 16384, 128
        return {
            "x": _w((lambda *s: torch.randn(*s, dtype=torch.bfloat16) * 0.05)(B, Sq, D)),
            "k_cache": _w((lambda *s: torch.randn(*s, dtype=torch.bfloat16) * 3.2)(B, H, Sk, Dh)),
            "v_cache": _w((lambda *s: torch.randn(*s, dtype=torch.bfloat16) * 0.4)(B, H, Sk, Dh)),
            "wq": _w((lambda *s: torch.randn(*s, dtype=torch.bfloat16) * 3.2)(D, H * Dh) * (D ** -0.5)),
            "wo": _w((lambda *s: torch.randn(*s, dtype=torch.bfloat16) * 0.4)(H * Dh, D) * ((H * Dh) ** -0.5)),
        }
    _A_in = _A_setup()

    def _B_setup():
        B, S, D, F = 1, 1, 512, 1792
        return {
            "x": _w((lambda *s: torch.randn(*s, dtype=torch.bfloat16) * 0.05)(B, S, D)),
            "w_gate": _w((lambda *s: torch.randn(*s, dtype=torch.bfloat16) * 0.05)(D, F)),
            "w_up": _w((lambda *s: torch.randn(*s, dtype=torch.bfloat16) * 0.05)(D, F)),
            "w_down": _w((lambda *s: torch.randn(*s, dtype=torch.bfloat16) * 0.05)(F, D)),
        }
    _B_in = _B_setup()

    return {
        "A_x": _A_in["x"],
        "A_k_cache": _A_in["k_cache"],
        "A_v_cache": _A_in["v_cache"],
        "A_wq": _A_in["wq"],
        "A_wo": _A_in["wo"],
        "B_x": _B_in["x"],
        "B_w_gate": _B_in["w_gate"],
        "B_w_up": _B_in["w_up"],
        "B_w_down": _B_in["w_down"],
    }

def reference(A_x, A_k_cache, A_v_cache, A_wq, A_wo, B_x, B_w_gate, B_w_up, B_w_down):
    def _A_ref(x, k_cache, v_cache, wq, wo):
        B, Sq, D = x.shape
        H, Dh = 32, 128
        q = (lambda x, *d: x.permute(*d))((x @ wq).reshape(B, Sq, H, Dh), 0, 2, 1, 3)
        scores = torch.einsum("bhqd,bhkd->bhqk", q, k_cache) * (Dh ** -0.5)
        p = _softmax(scores, axis=-1)
        o = torch.einsum("bhqk,bhkd->bhqd", p, v_cache)
        return (lambda x, *d: x.permute(*d))(o, 0, 2, 1, 3).reshape(B, Sq, H * Dh) @ wo

    def _B_ref(x, w_gate, w_up, w_down):
        g = _silu(x @ w_gate)
        u = x @ w_up
        return (g * u) @ w_down

    x = A_x
    h = x + _A_ref(_layernorm(x, axis=-1), A_k_cache, A_v_cache, A_wq, A_wo)
    h2 = h + _B_ref(_layernorm(h, axis=-1), B_w_gate, B_w_up, B_w_down)
    return h2
```

```cpp
#include <hip/hip_runtime.h>
#include <hip/hip_bf16.h>
#include <stdint.h>
#define MIMIC 0
typedef unsigned u32x4 __attribute__((ext_vector_type(4)));
typedef __bf16 bf16x2 __attribute__((ext_vector_type(2)));
constexpr int D = 512, H = 32, SK = 16384, DH = 128, F = 1792, NS = 8;
__device__ __forceinline__ float bf2f(unsigned short b) { return __builtin_bit_cast(float, (unsigned)b << 16); }
__device__ __forceinline__ float rbf(float f) { unsigned u = __builtin_bit_cast(unsigned, f); u = (u + 0x7fffu + ((u >> 16) & 1u)) & 0xffff0000u; return __builtin_bit_cast(float, u); }
__device__ __forceinline__ float mr(float f) { return MIMIC ? rbf(f) : f; }
__device__ __forceinline__ float dot2(unsigned k, unsigned q, float acc) { return __builtin_amdgcn_fdot2_f32_bf16(__builtin_bit_cast(bf16x2, k), __builtin_bit_cast(bf16x2, q), acc, false); }
__device__ float block_sum(float v, float* red, int nthr) {
  for (int o = 32; o > 0; o >>= 1) v += __shfl_xor(v, o);
  __syncthreads();
  if ((threadIdx.x & 63) == 0) red[threadIdx.x >> 6] = v;
  __syncthreads();
  float s = 0; for (int i = 0; i < nthr / 64; ++i) s += red[i];
  return s;
}
__device__ float ln512(float x, float* red) {
  float m = mr(block_sum(x, red, 512) * (1.f / 512));
  float mu = block_sum(x, red, 512) * (1.f / 512);
  float d = x - mu;
  float v = mr(block_sum(d * d, red, 512) * (1.f / 512));
  return mr(mr(x - m) / mr(sqrtf(mr(v + 1e-5f))));
}
__global__ void __launch_bounds__(512) k_attn(const unsigned short* x, const unsigned short* K, const unsigned short* V, const unsigned short* wq, float* part) {
  __shared__ float red[8]; __shared__ float xn[512]; __shared__ float qp[4][128]; __shared__ unsigned qb[64];
  __shared__ float mrg[8][132];
  const int h = blockIdx.x & 31, s = blockIdx.x >> 5, tid = threadIdx.x, lane = tid & 63, w = tid >> 6;
  xn[tid] = ln512(bf2f(x[tid]), red);
  __syncthreads();
  { int c = tid & 127, g = tid >> 7; float a = 0;
    for (int r = g * 128; r < g * 128 + 128; ++r) a += xn[r] * bf2f(wq[(size_t)r * 4096 + h * 128 + c]);
    qp[g][c] = a; }
  __syncthreads();
  if (tid < 64) { float a = qp[0][2*tid] + qp[1][2*tid] + qp[2][2*tid] + qp[3][2*tid]; float b = qp[0][2*tid+1] + qp[1][2*tid+1] + qp[2][2*tid+1] + qp[3][2*tid+1];
    qb[tid] = (__builtin_bit_cast(unsigned, rbf(a)) >> 16) | (__builtin_bit_cast(unsigned, rbf(b)) & 0xffff0000u); }
  __syncthreads();
  const int r = lane >> 4, c = lane & 15;
  unsigned q0 = qb[4*c], q1 = qb[4*c+1], q2 = qb[4*c+2], q3 = qb[4*c+3];
  const size_t base = ((size_t)h * SK + s * 2048 + w * 256) * DH;
  float m = -INFINITY, l = 0, acc[8] = {0,0,0,0,0,0,0,0};
  for (int t = 0; t < 256; t += 16) {
    u32x4 kk[4], vv[4];
    for (int i = 0; i < 4; ++i) { kk[i] = __builtin_nontemporal_load((const u32x4*)(K + base + (size_t)(t + 4*i + r) * DH + 8*c)); vv[i] = __builtin_nontemporal_load((const u32x4*)(V + base + (size_t)(t + 4*i + r) * DH + 8*c)); }
    float sc[4];
    for (int i = 0; i < 4; ++i) { float d = dot2(kk[i][0], q0, 0.f); d = dot2(kk[i][1], q1, d); d = dot2(kk[i][2], q2, d); d = dot2(kk[i][3], q3, d);
      d += __shfl_xor(d, 1); d += __shfl_xor(d, 2); d += __shfl_xor(d, 4); d += __shfl_xor(d, 8);
      sc[i] = mr(mr(d) * 0.08838834764831845f); }
    float mx = fmaxf(fmaxf(sc[0], sc[1]), fmaxf(sc[2], sc[3]));
    float mn = fmaxf(m, mx); float al = __expf(m - mn); m = mn; l *= al;
    for (int k = 0; k < 8; ++k) acc[k] *= al;
    for (int i = 0; i < 4; ++i) { float p = __expf(sc[i] - m); l += p;
      for (int k = 0; k < 4; ++k) { acc[2*k] += p * __builtin_bit_cast(float, vv[i][k] << 16); acc[2*k+1] += p * __builtin_bit_cast(float, vv[i][k] & 0xffff0000u); } }
  }
  for (int o = 16; o <= 32; o <<= 1) { float mo = __shfl_xor(m, o), lo = __shfl_xor(l, o); float mn = fmaxf(m, mo); float a = __expf(m - mn), b = __expf(mo - mn);
    l = l * a + lo * b; for (int k = 0; k < 8; ++k) { float ao = __shfl_xor(acc[k], o); acc[k] = acc[k] * a + ao * b; } m = mn; }
  if (r == 0) { for (int k = 0; k < 8; ++k) mrg[w][4 + 8*c + k] = acc[k]; if (c == 0) { mrg[w][0] = m; mrg[w][1] = l; } }
  __syncthreads();
  if (tid < 128) { float M = -INFINITY; for (int i = 0; i < 8; ++i) M = fmaxf(M, mrg[i][0]);
    float L = 0, o = 0; for (int i = 0; i < 8; ++i) { float e = __expf(mrg[i][0] - M); L += e * mrg[i][1]; o += e * mrg[i][4 + tid]; }
    float* p = part + (size_t)(h * NS + s) * 132; p[4 + tid] = o; if (tid == 0) { p[0] = M; p[1] = L; } }
}
__global__ void __launch_bounds__(512) k_wo(const float* part, const unsigned short* x, const unsigned short* wo, float* hout) {
  __shared__ float o[4096]; __shared__ float red[512][9];
  const int tid = threadIdx.x;
  for (int i = tid; i < 4096; i += 512) { int h = i >> 7, d = i & 127; const float* p = part + (size_t)h * NS * 132;
    float M = -INFINITY; for (int s = 0; s < NS; ++s) M = fmaxf(M, p[s * 132]);
    float L = 0, a = 0; for (int s = 0; s < NS; ++s) { float e = __expf(p[s*132] - M); L += e * p[s*132+1]; a += e * p[s*132 + 4 + d]; }
    o[i] = mr(a / L); }
  __syncthreads();
  float a[8] = {0,0,0,0,0,0,0,0};
  for (int r = tid; r < 4096; r += 512) { u32x4 w = *(const u32x4*)(wo + (size_t)r * 512 + blockIdx.x * 8); float ov = o[r];
    for (int k = 0; k < 4; ++k) { a[2*k] += ov * __builtin_bit_cast(float, w[k] << 16); a[2*k+1] += ov * __builtin_bit_cast(float, w[k] & 0xffff0000u); } }
  for (int k = 0; k < 8; ++k) red[tid][k] = a[k];
  __syncthreads();
  if (tid < 8) { float s = 0; for (int i = 0; i < 512; ++i) s += red[i][tid]; int c = blockIdx.x * 8 + tid; hout[c] = mr(bf2f(x[c]) + mr(s)); }
}
__global__ void __launch_bounds__(512) k_gu(const float* hin, const unsigned short* wg, const unsigned short* wu, float* act) {
  __shared__ float rs[8]; __shared__ float red[512][17];
  const int tid = threadIdx.x;
  float xn = ln512(hin[tid], rs);
  u32x4 g = *(const u32x4*)(wg + (size_t)tid * F + blockIdx.x * 8), u = *(const u32x4*)(wu + (size_t)tid * F + blockIdx.x * 8);
  for (int k = 0; k < 4; ++k) { red[tid][2*k] = xn * __builtin_bit_cast(float, g[k] << 16); red[tid][2*k+1] = xn * __builtin_bit_cast(float, g[k] & 0xffff0000u);
    red[tid][8 + 2*k] = xn * __builtin_bit_cast(float, u[k] << 16); red[tid][8 + 2*k+1] = xn * __builtin_bit_cast(float, u[k] & 0xffff0000u); }
  __syncthreads();
  if (tid < 8) { float a = 0, b = 0; for (int i = 0; i < 512; ++i) { a += red[i][tid]; b += red[i][8 + tid]; }
    a = mr(a); b = mr(b); float sg = mr(a / (1.f + __expf(-a))); act[blockIdx.x * 8 + tid] = mr(sg * b); }
}
__global__ void __launch_bounds__(256) k_down(const float* act, const float* hin, const unsigned short* wd, __hip_bfloat16* out) {
  __shared__ float red[256][9];
  const int tid = threadIdx.x;
  float a[8] = {0,0,0,0,0,0,0,0};
  for (int r = tid; r < F; r += 256) { u32x4 w = *(const u32x4*)(wd + (size_t)r * 512 + blockIdx.x * 8); float ov = act[r];
    for (int k = 0; k < 4; ++k) { a[2*k] += ov * __builtin_bit_cast(float, w[k] << 16); a[2*k+1] += ov * __builtin_bit_cast(float, w[k] & 0xffff0000u); } }
  for (int k = 0; k < 8; ++k) red[tid][k] = a[k];
  __syncthreads();
  if (tid < 8) { float s = 0; for (int i = 0; i < 256; ++i) s += red[i][tid]; int c = blockIdx.x * 8 + tid; out[c] = __float2bfloat16(hin[c] + mr(s)); }
}
extern "C" void kernel_launch(void* const* d_in, const int* in_sizes, int n_in, void* d_out, int out_size, void* d_ws, size_t ws_size, hipStream_t stream) {
  const unsigned short* x = (const unsigned short*)d_in[0]; const unsigned short* K = (const unsigned short*)d_in[1]; const unsigned short* V = (const unsigned short*)d_in[2];
  const unsigned short* wq = (const unsigned short*)d_in[3]; const unsigned short* wo = (const unsigned short*)d_in[4];
  const unsigned short* wg = (const unsigned short*)d_in[6]; const unsigned short* wu = (const unsigned short*)d_in[7]; const unsigned short* wd = (const unsigned short*)d_in[8];
  float* part = (float*)d_ws; float* hb = part + 65536; float* act = hb + 1024;
  k_attn<<<256, 512, 0, stream>>>(x, K, V, wq, part);
  k_wo<<<64, 512, 0, stream>>>(part, x, wo, hb);
  k_gu<<<224, 512, 0, stream>>>(hb, wg, wu, act);
  k_down<<<64, 256, 0, stream>>>(act, hb, wd, (__hip_bfloat16*)d_out);
}
```

```cpp
#include <hip/hip_runtime.h>
#include <hip/hip_bf16.h>
#include <stdint.h>
#include <stdio.h>

#define PACE_L 38.4
#define W0 8.0
#define W1 32.0
#define D1 110
#define D2 85
#define D3 100
#define E1 20
#define E2 20
#define E3 42

namespace {
typedef unsigned u32x4 __attribute__((ext_vector_type(4)));
typedef unsigned u32x3 __attribute__((ext_vector_type(3)));
typedef unsigned u32x2 __attribute__((ext_vector_type(2)));
typedef float f32x2 __attribute__((ext_vector_type(2)));
typedef float f32x4 __attribute__((ext_vector_type(4)));
typedef __bf16 bf16x2 __attribute__((ext_vector_type(2)));
typedef __amdgpu_buffer_rsrc_t rsrc_t;

constexpr int SC1 = 16, NT = 2;
constexpr unsigned OOB = 0x40000000u;
constexpr unsigned EMPTY = 0xffffffffu;
constexpr unsigned LSTR = 4352;
constexpr unsigned O_E = 0, O_ACT = 16384, O_Y = 65536, O_QP = 327680, O_PA = 589824, O_FLAG = 860160, O_GO = 1003520, O_ML = 1146880, WS_BYTES = 1290240;
constexpr int SPIN_MAX = 1 << 19;
constexpr int OFFQ = (int)(0.5 * 25600.0), ENDQ = (int)(PACE_L * 25600.0), TOTQ = ENDQ - OFFQ;
constexpr int QP_AT = 450, QP_ST = 25, FLAG_AT = 700, ALL_AT = 830, GO_AT = 1010, ML_AT = 500;
constexpr int WCX = 16, CATCH = 3, PRE = 4;
constexpr unsigned GOLD = 0x9e3779b1u;

__device__ __forceinline__ float asf(unsigned u) { return __builtin_bit_cast(float, u); }
__device__ __forceinline__ unsigned asu(float f) { return __builtin_bit_cast(unsigned, f); }
__device__ __forceinline__ unsigned pk(float lo, float hi) { f32x2 v = {lo, hi}; return __builtin_bit_cast(unsigned, __builtin_convertvector(v, bf16x2)); }
__device__ __forceinline__ float rbf(float f) { return asf(pk(f, f) << 16); }
__device__ __forceinline__ float lo16(unsigned u) { return asf(u << 16); }
__device__ __forceinline__ float hi16(unsigned u) { return asf(u & 0xffff0000u); }
__device__ __forceinline__ float dot2(unsigned a, unsigned b, float c) { return __builtin_amdgcn_fdot2_f32_bf16(__builtin_bit_cast(bf16x2, a), __builtin_bit_cast(bf16x2, b), c, false); }
__device__ __forceinline__ unsigned plo(unsigned a, unsigned b) { return __builtin_amdgcn_perm(b, a, 0x05040100u); }
__device__ __forceinline__ unsigned phi(unsigned a, unsigned b) { return __builtin_amdgcn_perm(b, a, 0x07060302u); }
template <int C> __device__ __forceinline__ float dpp(float v) { return asf((unsigned)__builtin_amdgcn_update_dpp(0, (int)asu(v), C, 0xf, 0xf, true)); }
constexpr int X8 = 0x128, X7 = 0x141, X2 = 0x4e, X1 = 0xb1, ROR4 = 0x124;
template <int C> __device__ __forceinline__ float merge(float a, float b, bool bit) { float k = bit ? b : a, s = bit ? a : b; return k + dpp<C>(s); }
__device__ __forceinline__ float sw32(float a, float b) { auto r = __builtin_amdgcn_permlane32_swap(asu(a), asu(b), false, false); return asf(r[0]) + asf(r[1]); }
__device__ __forceinline__ float sw16(float a, float b) { auto r = __builtin_amdgcn_permlane16_swap(asu(a), asu(b), false, false); return asf(r[0]) + asf(r[1]); }
__device__ __forceinline__ float row_sum(float v) { v += dpp<X8>(v); v += dpp<X7>(v); v += dpp<X2>(v); v += dpp<X1>(v); return v; }
__device__ __forceinline__ float oct_sum(float v) { v += dpp<X1>(v); v += dpp<X2>(v); v += dpp<X7>(v); return v; }
__device__ __forceinline__ float oct_max(float v) { v = fmaxf(v, dpp<X1>(v)); v = fmaxf(v, dpp<X2>(v)); return fmaxf(v, dpp<X7>(v)); }
__device__ __forceinline__ float wave_sum(float v) { v = row_sum(v); v = sw16(v, v); return sw32(v, v); }
__device__ __forceinline__ float wave_max(float v) {
  v = fmaxf(oct_max(v), dpp<X8>(oct_max(v)));
  { auto r = __builtin_amdgcn_permlane16_swap(asu(v), asu(v), false, false); v = fmaxf(asf(r[0]), asf(r[1])); }
  { auto r = __builtin_amdgcn_permlane32_swap(asu(v), asu(v), false, false); v = fmaxf(asf(r[0]), asf(r[1])); }
  return v;
}
__device__ __forceinline__ void pack_rows(float v, unsigned& p01, unsigned& p23) {
  auto r = __builtin_amdgcn_permlane16_swap(asu(v), asu(v), false, false);
  unsigned p = pk(asf(r[0]), asf(r[1]));
  auto q = __builtin_amdgcn_permlane32_swap(p, p, false, false);
  p01 = q[0]; p23 = q[1];
}
__device__ __forceinline__ float quad_cols(const float (&y)[4], bool b3, bool b2) {
  float t = merge<X7>(merge<X8>(y[0], y[1], b3), merge<X8>(y[2], y[3], b3), b2);
  t += dpp<X2>(t); t += dpp<X1>(t);
  return t;
}
__device__ __forceinline__ unsigned filled(float v) { return min(asu(v), EMPTY - 1u); }
__device__ __forceinline__ rsrc_t mk(const void* p, unsigned bytes) { return __builtin_amdgcn_make_buffer_rsrc((void*)p, 0, (int)bytes, 0x00020000); }
__device__ __forceinline__ unsigned now() { return (unsigned)__builtin_amdgcn_s_memrealtime(); }
__device__ __forceinline__ void lds_barrier() { asm volatile("s_waitcnt lgkmcnt(0)\n\ts_barrier" ::: "memory"); }
__device__ __forceinline__ bool any(bool p) { return __builtin_amdgcn_ballot_w64(p) != 0; }
__device__ __forceinline__ unsigned max8(const unsigned (&v)[8]) { return max(max(max(v[0], v[1]), max(v[2], v[3])), max(max(v[4], v[5]), max(v[6], v[7]))); }
__device__ __forceinline__ float sum8(const unsigned (&v)[8]) { return ((asf(v[0]) + asf(v[1])) + (asf(v[2]) + asf(v[3]))) + ((asf(v[4]) + asf(v[5])) + (asf(v[6]) + asf(v[7]))); }
__device__ __forceinline__ unsigned max4(const u32x4& v) { return max(max(v[0], v[1]), max(v[2], v[3])); }
__device__ __forceinline__ float share_sum(const u32x4& v, bool b3) {
  const float s0 = sw32(lo16(v[0]), lo16(v[2])), s1 = sw32(hi16(v[0]), hi16(v[2])), s2 = sw32(lo16(v[1]), lo16(v[3])), s3 = sw32(hi16(v[1]), hi16(v[3]));
  return merge<X8>(sw16(s0, s2), sw16(s1, s3), b3);
}
#define FENCE() asm volatile("" ::: "memory")
#define SB() __builtin_amdgcn_sched_barrier(0)
constexpr int bitrev4(int i) { return ((i & 1) << 3) | ((i & 2) << 1) | ((i & 4) >> 1) | ((i & 8) >> 3); }
constexpr float LOG2E = 1.4426950408889634f;
constexpr unsigned ONES = 0x3f803f80u;

struct alignas(16) Lds {
  u32x2 wo[8][64][4];
  u32x2 w2[7168];
  float a[8][128];
  float y[8][16];
  float g[20][8];
  float cs[16][8];
  float o[16][8];
  float hres[16];
  u32x4 x[64];
  unsigned q[64];
  u32x2 ml[10];
  float pad[3000];
};
__device__ __forceinline__ unsigned lds_addr(const void* p) { return (unsigned)(size_t)(const __attribute__((address_space(3))) void*)p; }
__device__ __forceinline__ u32x4 srd(const void* p, unsigned bytes) { const unsigned long long a = (unsigned long long)p; return (u32x4){(unsigned)a, (unsigned)(a >> 32) & 0xffffu, bytes, 0x00020000u}; }
__device__ __forceinline__ void dma(const u32x4& d, unsigned vo, unsigned so, unsigned dst) {
  unsigned keep;
  asm volatile("s_mov_b32 %0, m0\n\ts_mov_b32 m0, %1\n\ts_nop 0\n\tbuffer_load_dwordx4 %2, %3, %4 offen lds\n\ts_mov_b32 m0, %0" : "=&s"(keep) : "s"(dst), "v"(vo), "s"(d), "s"(so) : "memory");
}
template <int I> __device__ __forceinline__ void vstep(const u32x4& v, float p, float (&acc)[8]) {
  const float pb = dpp<0x150 + bitrev4(I)>(p);
#pragma unroll
  for (int k = 0; k < 4; ++k) { acc[2 * k] = fmaf(pb, lo16(v[k]), acc[2 * k]); acc[2 * k + 1] = fmaf(pb, hi16(v[k]), acc[2 * k + 1]); }
}
template <int N> struct IC { static constexpr int v = N; };
template <int I, int N, class F> __device__ __forceinline__ void unroll(F&& f) { if constexpr (I < N) { f(IC<I>{}); unroll<I + 1, N>(f); } }
__device__ __forceinline__ u32x4 pair_rows(const u32x2& a, const u32x2& b) { return (u32x4){plo(a[0], b[0]), phi(a[0], b[0]), plo(a[1], b[1]), phi(a[1], b[1])}; }

__global__ void __launch_bounds__(640, 3) fused_decode(const unsigned short* __restrict__ xin, const unsigned short* __restrict__ Kc, const unsigned short* __restrict__ Vc,
    const unsigned short* __restrict__ wq, const unsigned short* __restrict__ wo, const unsigned short* __restrict__ wg, const unsigned short* __restrict__ wu,
    const unsigned short* __restrict__ wd, unsigned short* __restrict__ out, unsigned char* __restrict__ ws) {
  __shared__ Lds L;
  unsigned t0 = now();
  asm volatile("" : "+s"(xin), "+s"(Kc), "+s"(Vc), "+s"(wq), "+s"(wo), "+s"(wg), "+s"(wu), "+s"(wd), "+s"(out), "+s"(ws));
  const int tid = threadIdx.x, lane = tid & 63, r = lane >> 4, c = lane & 15;
  const int w = __builtin_amdgcn_readfirstlane(tid >> 6);
  const int b = blockIdx.x, h = b & 31, s = b >> 5, hg = b & 7, cg = b >> 3;
  const bool ffn1 = cg < 28;
  const int wc = cg ^ WCX;
  const int fg = hg * 28 + cg, og = hg * 4 + (cg - 28);
  const bool b3 = lane & 8, b2 = lane & 4, b1 = lane & 2, b0 = lane & 1;
  const int qcol = 4 * r + (b3 ? 1 : 0) + (b2 ? 2 : 0);
  const rsrc_t rws = mk(ws, WS_BYTES);
  int spins = 0;
  unsigned long long pc; unsigned cend;
  asm volatile("s_getpc_b64 %0\n.Lcw_0:\n\ts_mov_b32 %1, .Lcw_1-.Lcw_0\n\ts_max_i32 %1, %1, .Lcw_2-.Lcw_0\n\ts_max_i32 %1, %1, 0" : "=&s"(pc), "=&s"(cend) :: "scc");
  const u32x4 rc = srd((const void*)pc, cend);
  asm volatile("s_cmp_lg_u32 %0, 9\n\ts_cbranch_scc1 1f\n\ts_mov_b32 m0, %1\n\ts_nop 0\n\tbuffer_load_dword %2, %3, 0 offen lds\n\tbuffer_load_dword %2, %3, %4 offen lds\n\tbuffer_load_dword %2, %3, %5 offen lds\n\tbuffer_load_dword %2, %3, %6 offen lds\n1:"
                       :: "s"(w), "s"(lds_addr(L.pad)), "v"(lane * 128), "s"(rc), "s"(8192), "s"(16384), "s"(24576) : "scc", "memory");
#define WAIT_UNTIL(tq) do { while ((int)((now() << 8) - (tq)) < 0) __builtin_amdgcn_s_sleep(1); } while (0)
#define SPIN_UNTIL(tq) do { const unsigned t_ = (tq); while ((int)((now() << 8) - t_) < 0) {} } while (0)

  if (__builtin_expect(w == 8, 1)) {
    const u32x4 empty4 = {EMPTY, EMPTY, EMPTY, EMPTY};
    const unsigned pa_off = O_PA + (h * 8 + s) * 256, y_off = O_Y + hg * 1024 + wc * 32;
    __builtin_amdgcn_s_setprio(3);
    const rsrc_t rx = mk(xin, 1024), rq = mk(wq, 4u << 20);
    const u32x4 xf = __builtin_amdgcn_raw_buffer_load_b128(rx, lane * 16, 0, 0);
    const u32x4 xp0 = __builtin_amdgcn_raw_buffer_load_b128(rx, r * 32, hg * 128, 0);
    const u32x4 xp1 = __builtin_amdgcn_raw_buffer_load_b128(rx, r * 32 + 16, hg * 128, 0);
    SB();
    u32x4 wqv[16];
#pragma unroll
    for (int i = 0; i < 16; ++i) wqv[i] = __builtin_amdgcn_raw_buffer_load_b128(rq, r * (16 * 8192) + c * 16, (hg * 64 + i) * 8192 + cg * 256, NT);
    const u32x2 ew = __builtin_amdgcn_raw_buffer_load_b64(rws, O_E, 0, 0);
    SB();
    if (lane < 10) L.ml[lane] = (u32x2){EMPTY, EMPTY};
    L.q[lane] = EMPTY;
    lds_barrier();
    __builtin_amdgcn_raw_buffer_store_b128(empty4, rws, lane < 16 ? pa_off + lane * 16 : lane == 16 ? O_ML + h * LSTR + s * 16 : (lane == 24 && ffn1) ? O_ACT + fg * 16
                                                        : (lane >= 32 && lane < 34) ? y_off + (lane & 1) * 16 : OOB, 0, SC1);
    SB();
    L.x[lane] = xf;
    unsigned xn[8];
    {
      float s1 = 0.f;
#pragma unroll
      for (int k = 0; k < 4; ++k) s1 += lo16(xf[k]) + hi16(xf[k]);
      const float mu = wave_sum(s1) * (1.f / 512);
      float s2 = 0.f;
#pragma unroll
      for (int k = 0; k < 4; ++k) { const float a = lo16(xf[k]) - mu, d = hi16(xf[k]) - mu; s2 = fmaf(a, a, s2); s2 = fmaf(d, d, s2); }
      const float var = rbf(wave_sum(s2) * (1.f / 512));
      const float m = rbf(mu), rs = 1.f / rbf(sqrtf(rbf(var + 1e-5f)));
#pragma unroll
      for (int k = 0; k < 8; ++k) { const unsigned u = k < 4 ? xp0[k & 3] : xp1[k & 3]; const unsigned d = pk(lo16(u) - m, hi16(u) - m); xn[k] = pk(lo16(d) * rs, hi16(d) * rs); }
    }
    asm volatile("" :: "v"(xn[0]), "v"(xn[7]));
    float a[8];
#pragma unroll
    for (int k = 0; k < 8; ++k) a[k] = 0.f;
#pragma unroll
    for (int j = 0; j < 8; ++j)
#pragma unroll
      for (int k = 0; k < 4; ++k) {
        a[2 * k] = dot2(plo(wqv[2 * j][k], wqv[2 * j + 1][k]), xn[j], a[2 * k]);
        a[2 * k + 1] = dot2(phi(wqv[2 * j][k], wqv[2 * j + 1][k]), xn[j], a[2 * k + 1]);
      }
    const unsigned tlo = ew[0] + 1u, thi = ew[1] * 747796405u + ew[0] + 2891336453u;
    {
      const float s0 = sw32(a[0], a[4]), s1 = sw32(a[1], a[5]), s2 = sw32(a[2], a[6]), s3 = sw32(a[3], a[7]);
      __builtin_amdgcn_raw_buffer_store_b64((u32x2){asu(sw16(s0, s2)), tlo}, rws, lane * 8, O_QP + (cg * 8 + hg) * 1024, SC1);
      __builtin_amdgcn_raw_buffer_store_b64((u32x2){asu(sw16(s1, s3)), tlo}, rws, lane * 8 + 512, O_QP + (cg * 8 + hg) * 1024, SC1);
    }
    WAIT_UNTIL((t0 + QP_AT + s * QP_ST) << 8);
    {
      u32x2 v[16];
#pragma unroll 1
      for (;;) {
        FENCE();
#pragma unroll
        for (int u = 0; u < 16; ++u) v[u] = __builtin_amdgcn_raw_buffer_load_b64(rws, lane * 8 + (u & 1) * 512, O_QP + h * 8192 + (u >> 1) * 1024, SC1);
        unsigned bad = 0;
#pragma unroll
        for (int u = 0; u < 16; ++u) bad |= v[u][1] ^ tlo;
        if (!any(bad != 0) || ++spins > SPIN_MAX) break;
        __builtin_amdgcn_s_sleep(30);
      }
      unsigned e[8], o[8];
#pragma unroll
      for (int u = 0; u < 8; ++u) { e[u] = v[2 * u][0]; o[u] = v[2 * u + 1][0]; }
      L.q[4 * c + r] = min(pk(sum8(e), sum8(o)), EMPTY - 1u);
    }
    unsigned te;
    {
      WAIT_UNTIL((t0 + FLAG_AT) << 8);
      asm volatile("s_waitcnt vmcnt(0)" ::: "memory");
      __builtin_amdgcn_raw_buffer_store_b96((u32x3){tlo ^ ((unsigned)b * GOLD), thi, t0}, rws, lane == 0 ? O_FLAG + cg * LSTR + hg * 16 : OOB, 0, SC1);
      if (b == 0) {
        WAIT_UNTIL((t0 + ALL_AT) << 8);
        u32x3 f[4];
#pragma unroll 1
        for (;;) {
          FENCE();
#pragma unroll
          for (int u = 0; u < 4; ++u) f[u] = __builtin_amdgcn_raw_buffer_load_b96(rws, (lane >> 3) * LSTR + (lane & 7) * 16 + u * (8 * LSTR), O_FLAG, SC1);
          unsigned bad = 0;
#pragma unroll
          for (int u = 0; u < 4; ++u) bad |= (f[u][0] ^ tlo ^ ((unsigned)(lane + 64 * u) * GOLD)) | (f[u][1] ^ thi);
          if (!any(bad != 0) || ++spins > SPIN_MAX) break;
          __builtin_amdgcn_s_sleep(16);
        }
        FENCE();
        __builtin_amdgcn_raw_buffer_store_b64((u32x2){tlo, thi}, rws, lane == 0 ? O_E : OOB, 0, SC1);
        const int d = max(max((int)(f[0][2] - t0), (int)(f[1][2] - t0)), max((int)(f[2][2] - t0), (int)(f[3][2] - t0)));
        const unsigned last = t0 + (unsigned)__builtin_amdgcn_readfirstlane((int)wave_max((float)min(d, 300)));
        __builtin_amdgcn_raw_buffer_store_b96((u32x3){~tlo, thi + 1u, last}, rws, lane < 32 ? O_GO + lane * LSTR : OOB, 0, SC1);
      }
      WAIT_UNTIL((t0 + GO_AT) << 8);
      u32x3 g;
#pragma unroll 1
      for (;;) {
        FENCE();
        g = __builtin_amdgcn_raw_buffer_load_b96(rws, 0, O_GO + cg * LSTR, SC1);
        if ((__builtin_amdgcn_readfirstlane(g[0]) == ~tlo && __builtin_amdgcn_readfirstlane(g[1]) == thi + 1u) || ++spins > SPIN_MAX) break;
        __builtin_amdgcn_s_sleep(24);
      }
      FENCE();
      te = min(((t0 + min(max((int)(__builtin_amdgcn_readfirstlane(g[2]) - t0), 0), 300)) << 8) + ENDQ, EMPTY - 1u);
      if (lane == 0) L.ml[9][0] = te;
    }
    {
      u32x2 v;
#pragma unroll 1
      for (;;) {
        FENCE();
        v = L.ml[lane & 7];
        if (!any(max(v[0], v[1]) == EMPTY) || ++spins > SPIN_MAX) break;
        __builtin_amdgcn_s_sleep(2);
      }
      const float M = oct_max(asf(v[0]));
      const float l = oct_sum(asf(v[1]) * __builtin_amdgcn_exp2f((asf(v[0]) - M) * LOG2E));
      __builtin_amdgcn_raw_buffer_store_b64((u32x2){filled(M), filled(l)}, rws, lane == 0 ? O_ML + h * LSTR + s * 16 : OOB, 0, SC1);
    }
    float hM, hl;
    {
      u32x2 v;
      WAIT_UNTIL(te - ML_AT * 256);
#pragma unroll 1
      for (;;) {
        FENCE();
        v = __builtin_amdgcn_raw_buffer_load_b64(rws, (lane & 7) * 16, O_ML + h * LSTR, SC1);
        if (!any(max(v[0], v[1]) == EMPTY) || ++spins > SPIN_MAX) break;
        __builtin_amdgcn_s_sleep(40);
      }
      hM = oct_max(asf(v[0]));
      hl = 1.f / oct_sum(asf(v[1]) * __builtin_amdgcn_exp2f((asf(v[0]) - hM) * LOG2E));
    }
    if (lane == 0) L.ml[8] = (u32x2){filled(hM), filled(hl)};
    lds_barrier();
    {
      f32x2 v[8];
#pragma unroll
      for (int u = 0; u < 8; ++u) v[u] = *(const f32x2*)&L.a[u][2 * lane];
      const f32x2 t = ((v[0] + v[1]) + (v[2] + v[3])) + ((v[4] + v[5]) + (v[6] + v[7]));
      __builtin_amdgcn_raw_buffer_store_b32(min(pk(t[0], t[1]), EMPTY - 1u), rws, lane * 4, pa_off, SC1);
    }
    lds_barrier();
    {
      f32x2 t = *(const f32x2*)&L.y[lane >> 3][2 * (lane & 7)];
      t[0] += dpp<X8>(t[0]); t[1] += dpp<X8>(t[1]);
      t[0] = sw16(t[0], t[0]); t[1] = sw16(t[1], t[1]);
      t[0] = sw32(t[0], t[0]); t[1] = sw32(t[1], t[1]);
      const unsigned py = min(pk(t[0], t[1]), EMPTY - 1u);
      __builtin_amdgcn_raw_buffer_store_b32(py, rws, lane < 8 ? lane * 4 : OOB, y_off, SC1);
    }
    if (ffn1) {
      const float csg = oct_sum(L.cs[lane >> 3][lane & 7]), csu = oct_sum(L.cs[8 + (lane >> 3)][lane & 7]);
      asm volatile("" :: "v"(csg), "v"(csu));
      lds_barrier();
      float g = L.g[lane >> 3][lane & 7], u = L.g[8 + (lane >> 3)][lane & 7], s1 = L.g[16][lane & 7], s2 = L.g[17][lane & 7];
      s1 += dpp<X1>(s1); s2 += dpp<X1>(s2); g += dpp<X1>(g); u += dpp<X1>(u);
      s1 += dpp<X2>(s1); s2 += dpp<X2>(s2); g += dpp<X2>(g); u += dpp<X2>(u);
      s1 += dpp<X7>(s1); s2 += dpp<X7>(s2); g += dpp<X7>(g); u += dpp<X7>(u);
      const float mu = s1 * (1.f / 512);
      const float var = rbf(fmaxf(fmaf(-mu, mu, s2 * (1.f / 512)), 0.f));
      const float m = rbf(mu), rs = __builtin_amdgcn_rcpf(rbf(__builtin_amdgcn_sqrtf(rbf(var + 1e-5f))));
      g = rbf(fmaf(-m, csg, g) * rs); u = rbf(fmaf(-m, csu, u) * rs);
      const float sg = rbf(g * __builtin_amdgcn_rcpf(1.f + __builtin_amdgcn_exp2f(-g * LOG2E)));
      const float a = sg * u;
      const unsigned pa = min(pk(a, dpp<X8>(a)), EMPTY - 1u);
      __builtin_amdgcn_raw_buffer_store_b32(pa, rws, c == 0 ? r * 4 : OOB, O_ACT + fg * 16, SC1);
      asm volatile(".Lcw_1:");
    } else {
      lds_barrier();
      const f32x2 hh = *(const f32x2*)&L.hres[2 * (lane >> 3)];
      const float h0 = rbf(hh[0]), h1 = rbf(hh[1]);
      lds_barrier();
      const float d0 = oct_sum(L.o[2 * (lane >> 3)][lane & 7]), d1 = oct_sum(L.o[2 * (lane >> 3) + 1][lane & 7]);
      unsigned po = pk(h0 + rbf(d0), h1 + rbf(d1));
      if (spins > SPIN_MAX) po = 0x7fc07fc0u;
      __builtin_amdgcn_raw_buffer_store_b32(po, mk(out, 1024), (lane & 7) == 0 ? og * 32 + (lane >> 3) * 4 : OOB, 0, 0);
    }
  } else if (w < 8) {
    unsigned dtq = ffn1 ? TOTQ / 65 : TOTQ / 49;
    unsigned tq = (t0 << 8) + OFFQ + w * (dtq >> 3);
#define PACE() do { WAIT_UNTIL(tq); tq += dtq; } while (0)
    const unsigned kvbase = (unsigned)h * (16384u * 256u) + s * (66u * 8192u) + w * 1024u;
    const rsrc_t rk = mk(Kc, 128u << 20), rv = mk(Vc, 128u << 20);
    const unsigned voff = lane * 16;
    u32x4 mk_[2], mv_[2], ext[16], buf[16];
    unroll<0, 36>([&](auto D) {
      constexpr int d = D.v;
      if (d % 2 == 0) PACE();
      if (d < 2) mk_[d & 1] = __builtin_amdgcn_raw_buffer_load_b128(rk, voff, kvbase + d * 8192, NT);
      else if (d < 4) mv_[d & 1] = __builtin_amdgcn_raw_buffer_load_b128(rv, voff, kvbase + (d - 2) * 8192, NT);
      else (d < 20 ? ext[(d - 4) & 15] : buf[(d - 4) & 15]) = __builtin_amdgcn_raw_buffer_load_b128(rk, voff, kvbase + (d - 2) * 8192, NT);
      SB();
      if (d == 1) lds_barrier();
    });
    u32x4 q;
#pragma unroll 1
    for (;;) {
      FENCE();
      q = *(const u32x4*)&L.q[4 * c];
      if (!any(max(max(q[0], q[1]), max(q[2], q[3])) == EMPTY) || ++spins > SPIN_MAX) break;
      __builtin_amdgcn_s_sleep(1);
    }
    unsigned te = (t0 << 8) + ENDQ;
    int rem = ffn1 ? 48 : 32;
    const unsigned lead = (7 - w) * (dtq >> 3);
    auto plan = [&]() {
      const unsigned e = __builtin_amdgcn_readfirstlane(L.ml[9][0]);
      if (e != EMPTY) te = e;
      { const unsigned n = now() << 8; if ((int)(n - tq) > 0) tq = n; }
      const int left = max((int)(te - lead - tq), 0);
      const float inv = __builtin_amdgcn_rcpf((float)(rem - 1));
      dtq = (unsigned)__builtin_amdgcn_readfirstlane((int)((float)left * inv));
      rem -= 8;
    };
    float tp, l1, l2, l3, res;
    auto kdot = [&](const u32x4& k) { float t = dot2(k[0], q[0], 0.f); t = dot2(k[1], q[1], t); t = dot2(k[2], q[2], t); t = dot2(k[3], q[3], t); asm volatile("" : "+v"(t)); SB(); return t; };
    auto kmerge = [&](auto D, float t) {
      constexpr int d = D.v;
      if (d % 2 == 0) tp = t;
      else {
        t = merge<X8>(tp, t, b3);
        if (d % 4 == 1) l1 = t;
        else {
          t = merge<X7>(l1, t, b2);
          if (d % 8 == 3) l2 = t;
          else {
            t = merge<X2>(l2, t, b1);
            if (d == 7) l3 = t; else res = merge<X1>(l3, t, b0);
          }
        }
      }
      SB();
    };
    auto score = [](float v) { return rbf(rbf(v) * 0.08838834764831845f); };
    float scm;
    { const float ta = kdot(mk_[0]), tc = kdot(mk_[1]); scm = score(oct_sum(merge<X8>(ta, tc, b3))); }
    unroll<0, 16>([&](auto D) { kmerge(D, kdot(ext[D.v])); });
    float sc0 = -3.0e38f, sc1 = -3.0e38f, sc2 = -3.0e38f, sc3 = score(res);
    unsigned ko = kvbase + 34u * 8192u;
#pragma unroll 1
    for (int it = ffn1 ? 1 : 2; it < 4; ++it) {
      const rsrc_t rn = mk(it == 3 ? Vc : Kc, 128u << 20);
      const unsigned so = it == 3 ? kvbase + 2u * 8192u : ko;
      ko += 16u * 8192u;
      plan();
      unroll<0, 16>([&](auto D) {
        constexpr int d = D.v;
        const float t = kdot(buf[d]);
        if (d % 2 == 0) PACE();
        buf[d] = __builtin_amdgcn_raw_buffer_load_b128(rn, voff, so + d * 8192, NT);
        kmerge(D, t);
      });
      sc0 = sc1; sc1 = sc2; sc2 = sc3;
      sc3 = score(res);
    }
    const float mw = fmaxf(wave_max(fmaxf(fmaxf(fmaxf(sc0, sc1), fmaxf(sc2, sc3)), scm)), -3.0e38f);
    const float p0 = ffn1 ? __builtin_amdgcn_exp2f((sc0 - mw) * LOG2E) : 0.f, p1 = __builtin_amdgcn_exp2f((sc1 - mw) * LOG2E);
    const float p2 = __builtin_amdgcn_exp2f((sc2 - mw) * LOG2E), p3 = __builtin_amdgcn_exp2f((sc3 - mw) * LOG2E);
    const float pm = __builtin_amdgcn_exp2f((scm - mw) * LOG2E);
    {
      const float lw = wave_sum((p0 + p1) + (p2 + p3) + ((lane & 7) == 0 ? pm : 0.f));
      if (lane == 0) L.ml[w] = (u32x2){filled(mw), filled(lw)};
    }
    SB();
    float acc[8];
#pragma unroll
    for (int k = 0; k < 8; ++k) acc[k] = 0.f;
#define PIN_ACC() asm volatile("" : "+v"(acc[0]), "+v"(acc[1]), "+v"(acc[2]), "+v"(acc[3]), "+v"(acc[4]), "+v"(acc[5]), "+v"(acc[6]), "+v"(acc[7]))
    vstep<0>(mv_[0], pm, acc); vstep<1>(mv_[1], pm, acc);
    PIN_ACC(); SB();
    float pc = ffn1 ? p0 : p1;
    ko = kvbase + 18u * 8192u;
#pragma unroll 1
    for (int it = ffn1 ? 0 : 1; it < 2; ++it) {
      plan();
      unroll<0, 16>([&](auto D) {
        constexpr int d = D.v;
        vstep<d>(buf[d], pc, acc);
        PIN_ACC(); SB();
        if (d % 2 == 0) PACE();
        buf[d] = __builtin_amdgcn_raw_buffer_load_b128(rv, voff, ko + d * 8192, NT);
        SB();
      });
      pc = p1; ko += 16u * 8192u;
    }
    float cw, xo;
    u32x4 wo0, wo1, wv[7];
    plan();
    unroll<0, 16>([&](auto D) {
      constexpr int d = D.v;
      vstep<d>(buf[d], p2, acc);
      PIN_ACC(); SB();
      if (d % 2 == 0) PACE();
      buf[d] = __builtin_amdgcn_raw_buffer_load_b128(rv, voff, ko + d * 8192, NT);
      SB();
      if constexpr (d % 2 == 1 && d > 2 * CATCH) { vstep<d - 2 * CATCH - 1>(buf[d - 2 * CATCH - 1], p3, acc); PIN_ACC(); SB(); vstep<d - 2 * CATCH>(buf[d - 2 * CATCH], p3, acc); PIN_ACC(); SB(); }
      if constexpr (d == 2 * PRE + 1) {
        u32x2 v;
#pragma unroll 1
        for (;;) {
          FENCE();
          v = L.ml[8];
          if (!any(max(max(v[0], v[1]), L.ml[9][1]) == EMPTY) || ++spins > SPIN_MAX) break;
        }
        cw = __builtin_amdgcn_exp2f((mw - asf(v[0])) * LOG2E) * asf(v[1]);
        xo = asf((unsigned)((const unsigned short*)L.x)[w * 64 + 8 * (c & 7) + 2 * r + (c >> 3)] << 16);
        wo0 = pair_rows(L.wo[w][c][r], L.wo[w][16 + c][r]);
        wo1 = pair_rows(L.wo[w][32 + c][r], L.wo[w][48 + c][r]);
        if (ffn1) {
          const u32x2* g = &L.w2[(r >> 1) * 1024 + (w * 64 + c) * 2 + (r & 1)];
          wv[0] = pair_rows(g[0], g[32]); wv[1] = pair_rows(g[64], g[96]);
          float y[4];
#pragma unroll
          for (int n = 0; n < 4; ++n) y[n] = dot2(wv[0][n], ONES, dot2(wv[1][n], ONES, 0.f));
          const float t = quad_cols(y, b3, b2);
          if ((lane & 3) == 0) L.cs[qcol][w] = t;
#pragma unroll
          for (int j = 2; j < 7; ++j) wv[j] = wv[0];
        } else {
#pragma unroll
          for (int j = 0; j < 7; ++j) wv[j] = pair_rows(L.w2[(w * 7 + j) * 128 + c * 4 + r], L.w2[(w * 7 + j) * 128 + 64 + c * 4 + r]);
        }
        asm volatile("" : "+v"(wo0), "+v"(wo1), "+v"(cw), "+v"(xo), "+v"(wv[0]), "+v"(wv[1]), "+v"(wv[2]), "+v"(wv[3]), "+v"(wv[4]), "+v"(wv[5]), "+v"(wv[6]));
        SB();
      }
    });
    unroll<16 - 2 * CATCH, 16>([&](auto D) {
      constexpr int d = D.v;
      vstep<d>(buf[d], p3, acc);
      PIN_ACC(); SB();
    });
    {
      const float s0 = sw32(acc[0], acc[4]), s1 = sw32(acc[1], acc[5]), s2 = sw32(acc[2], acc[6]), s3 = sw32(acc[3], acc[7]);
      *(f32x2*)&L.a[w][8 * c + 2 * r] = (f32x2){sw16(s0, s2) * cw, sw16(s1, s3) * cw};
    }
    lds_barrier();
    te = __builtin_amdgcn_readfirstlane(L.ml[9][0]);
    {
      u32x4 v;
      const unsigned so = O_PA + (hg + 8 * (w >> 1)) * 2048 + (w & 1) * 128;
      SPIN_UNTIL((now() + E1) << 8); SPIN_UNTIL(te + D1 * 256);
#pragma unroll 1
      for (;;) {
        FENCE();
        v = __builtin_amdgcn_raw_buffer_load_b128(rws, (lane >> 3) * 256 + (lane & 7) * 16, so, SC1);
        if (!any(max4(v) == EMPTY) || ++spins > SPIN_MAX) break;
      }
      unsigned p01, p23; pack_rows(share_sum(v, b3), p01, p23);
      float y[4];
#pragma unroll
      for (int n = 0; n < 4; ++n) y[n] = dot2(wo0[n], p01, dot2(wo1[n], p23, 0.f));
      const float t = quad_cols(y, b3, b2);
      if ((lane & 3) == 0) L.y[w][qcol] = t;
    }
    lds_barrier();
    float hv;
    {
      u32x4 v;
      SPIN_UNTIL((now() + E2) << 8); SPIN_UNTIL(te + (D1 + D2) * 256);
#pragma unroll 1
      for (;;) {
        FENCE();
        v = __builtin_amdgcn_raw_buffer_load_b128(rws, (lane >> 3) * 1024 + (lane & 7) * 16, O_Y + w * 128, SC1);
        if (!any(max4(v) == EMPTY) || ++spins > SPIN_MAX) break;
      }
      hv = xo + rbf(share_sum(v, b3));
    }
    if (ffn1) {
      unsigned p01, p23; pack_rows(hv, p01, p23);
      float y[4];
#pragma unroll
      for (int n = 0; n < 4; ++n) y[n] = dot2(wv[0][n], p01, dot2(wv[1][n], p23, 0.f));
      float z = dot2(p01, (r & 1) ? p01 : ONES, dot2(p23, (r & 1) ? p23 : ONES, 0.f));
      const float t = quad_cols(y, b3, b2);
      z = row_sum(z);
      if ((lane & 3) == 0) L.g[qcol][w] = t;
      if (c == 1) L.g[16 + r][w] = z;
      lds_barrier();
      asm volatile(".Lcw_2:");
    } else {
      u32x4 va; u32x3 vb;
      if (w * 4 + ((c & 7) >> 1) == og) L.hres[8 * (c & 1) + 2 * r + (c >> 3)] = hv;
      lds_barrier();
      SPIN_UNTIL((now() + E3) << 8); SPIN_UNTIL(te + (D1 + D2 + D3) * 256);
#pragma unroll 1
      for (;;) {
        FENCE();
        va = __builtin_amdgcn_raw_buffer_load_b128(rws, c * 28, O_ACT + w * 448, SC1);
        vb = __builtin_amdgcn_raw_buffer_load_b96(rws, c * 28 + 16, O_ACT + w * 448, SC1);
        if (!any(max(max(max(va[0], va[1]), max(va[2], va[3])), max(max(vb[0], vb[1]), vb[2])) == EMPTY) || ++spins > SPIN_MAX) break;
      }
      float y[4];
#pragma unroll
      for (int n = 0; n < 4; ++n) {
        y[n] = dot2(wv[0][n], va[0], 0.f); y[n] = dot2(wv[1][n], va[1], y[n]); y[n] = dot2(wv[2][n], va[2], y[n]); y[n] = dot2(wv[3][n], va[3], y[n]);
        y[n] = dot2(wv[4][n], vb[0], y[n]); y[n] = dot2(wv[5][n], vb[1], y[n]); y[n] = dot2(wv[6][n], vb[2], y[n]);
      }
      const float t = quad_cols(y, b3, b2);
      if ((lane & 3) == 0) L.o[qcol][w] = t;
      lds_barrier();
    }
  } else {
    lds_barrier();
    const u32x4 ro = srd(wo, 4u << 20), r1 = ffn1 ? srd(wg, 512 * 3584) : srd(wd, 1792 * 1024), r2 = srd(wu, 512 * 3584);
    const unsigned vo = (8 * ((lane >> 1) & 7) + ((lane >> 4) & 1) + 2 * (lane >> 5)) * 1024 + (lane & 1) * 16;
    const unsigned vg = (8 * (lane & 7) + ((lane >> 3) & 1) + 2 * (lane >> 4)) * 3584;
    const unsigned vd = ((lane >> 1) & 15) * (14 * 1024) + (lane >> 5) * 1024 + (lane & 1) * 16;
    unsigned th = (t0 << 8) + (int)(W0 * 25600);
    constexpr int WQ = (int)((W1 - W0) * 25600);
    const unsigned dth = ffn1 ? WQ / 31 : WQ / 71;
#pragma unroll 1
    for (int n = 0; n < (ffn1 ? 32 : 72); ++n) {
      WAIT_UNTIL(th); th += dth;
      if (n < 16) {
        const int t = n ^ (cg & 3);
        dma(ro, vo, ((hg + 8 * (t >> 2)) * 128 + (t & 2) * 32 + (t & 1) * 4) * 1024 + wc * 32, lds_addr(L.wo) + t * 1024);
      } else if (ffn1) {
        const int g = (n - 16) ^ (fg & 7);
        dma((g & 8) ? r2 : r1, vg, (g & 7) * (64 * 3584) + fg * 16, lds_addr(L.w2) + g * 1024);
      } else {
        const int d = (n - 16) ^ (og & 3);
        dma(r1, vd, (d / 7 * 224 + d % 7 * 2) * 1024 + og * 32, lds_addr(L.w2) + d * 1024);
      }
    }
    asm volatile("s_waitcnt vmcnt(0)" ::: "memory");
    if (lane == 0) L.ml[9][1] = 0u;
  }
}
}

extern "C" void kernel_launch(void* const* d_in, const int* in_sizes, int n_in, void* d_out, int out_size, void* d_ws, size_t ws_size, hipStream_t stream) {
  for (int i = 0; i < 9; ++i) fprintf(stderr, "in%d %p\n", i, d_in[i]);
  fprintf(stderr, "out %p ws %p\n", d_out, d_ws);
  fused_decode<<<256, 640, 0, stream>>>((const unsigned short*)d_in[0], (const unsigned short*)d_in[1], (const unsigned short*)d_in[2], (const unsigned short*)d_in[3],
      (const unsigned short*)d_in[4], (const unsigned short*)d_in[6], (const unsigned short*)d_in[7], (const unsigned short*)d_in[8], (unsigned short*)d_out, (unsigned char*)d_ws);
}
```

```cpp
#include <hip/hip_runtime.h>
#include <hip/hip_bf16.h>
#include <stdint.h>

namespace {
typedef unsigned u32x4 __attribute__((ext_vector_type(4)));
typedef unsigned u32x3 __attribute__((ext_vector_type(3)));
typedef unsigned u32x2 __attribute__((ext_vector_type(2)));
typedef float f32x2 __attribute__((ext_vector_type(2)));
typedef float f32x4 __attribute__((ext_vector_type(4)));
typedef __bf16 bf16x2 __attribute__((ext_vector_type(2)));
typedef __amdgpu_buffer_rsrc_t rsrc_t;

constexpr int SC1 = 16, NT = 2;
constexpr unsigned OOB = 0x40000000u;
constexpr unsigned EMPTY = 0xffffffffu;
constexpr unsigned LSTR = 4352;
constexpr unsigned O_E = 0, O_ACT = 16384, O_Y = 65536, O_QP = 327680, O_PA = 589824, O_FLAG = 860160, O_GO = 1003520, O_ML = 1146880, WS_BYTES = 1290240;
constexpr int SPIN_MAX = 1 << 19;
constexpr int OFFQ = (int)(0.5 * 25600.0), ENDQ = (int)(37.8 * 25600.0), TOTQ = ENDQ - OFFQ;
constexpr int FAST = 16, W0 = 8 * 25600, W1 = 32 * 25600, WPRE = 7 * 25600;
constexpr int WM = 16 * 25600, WE = 30 * 25600;
constexpr float SLOW = 1.08f;
constexpr int D1 = 110, D2 = 85, D3 = 100, E1 = 20, E2 = 20, E3 = 42;
constexpr int QP_AT = 450, QP_ST = 25, FLAG_AT = 700, ALL_AT = 830, GO_AT = 1010, ML_AT = 500;
constexpr int WCX = 16, CATCH = 3, PRE = 4;
constexpr unsigned GOLD = 0x9e3779b1u;

__device__ __forceinline__ float asf(unsigned u) { return __builtin_bit_cast(float, u); }
__device__ __forceinline__ unsigned asu(float f) { return __builtin_bit_cast(unsigned, f); }
__device__ __forceinline__ unsigned pk(float lo, float hi) { f32x2 v = {lo, hi}; return __builtin_bit_cast(unsigned, __builtin_convertvector(v, bf16x2)); }
__device__ __forceinline__ float rbf(float f) { return asf(pk(f, f) << 16); }
__device__ __forceinline__ float lo16(unsigned u) { return asf(u << 16); }
__device__ __forceinline__ float hi16(unsigned u) { return asf(u & 0xffff0000u); }
__device__ __forceinline__ float dot2(unsigned a, unsigned b, float c) { return __builtin_amdgcn_fdot2_f32_bf16(__builtin_bit_cast(bf16x2, a), __builtin_bit_cast(bf16x2, b), c, false); }
__device__ __forceinline__ unsigned plo(unsigned a, unsigned b) { return __builtin_amdgcn_perm(b, a, 0x05040100u); }
__device__ __forceinline__ unsigned phi(unsigned a, unsigned b) { return __builtin_amdgcn_perm(b, a, 0x07060302u); }
template <int C> __device__ __forceinline__ float dpp(float v) { return asf((unsigned)__builtin_amdgcn_update_dpp(0, (int)asu(v), C, 0xf, 0xf, true)); }
constexpr int X8 = 0x128, X7 = 0x141, X2 = 0x4e, X1 = 0xb1, ROR4 = 0x124;
template <int C> __device__ __forceinline__ float merge(float a, float b, bool bit) { float k = bit ? b : a, s = bit ? a : b; return k + dpp<C>(s); }
__device__ __forceinline__ float sw32(float a, float b) { auto r = __builtin_amdgcn_permlane32_swap(asu(a), asu(b), false, false); return asf(r[0]) + asf(r[1]); }
__device__ __forceinline__ float sw16(float a, float b) { auto r = __builtin_amdgcn_permlane16_swap(asu(a), asu(b), false, false); return asf(r[0]) + asf(r[1]); }
__device__ __forceinline__ float row_sum(float v) { v += dpp<X8>(v); v += dpp<X7>(v); v += dpp<X2>(v); v += dpp<X1>(v); return v; }
__device__ __forceinline__ float oct_sum(float v) { v += dpp<X1>(v); v += dpp<X2>(v); v += dpp<X7>(v); return v; }
__device__ __forceinline__ float oct_max(float v) { v = fmaxf(v, dpp<X1>(v)); v = fmaxf(v, dpp<X2>(v)); return fmaxf(v, dpp<X7>(v)); }
__device__ __forceinline__ float wave_sum(float v) { v = row_sum(v); v = sw16(v, v); return sw32(v, v); }
__device__ __forceinline__ float wave_max(float v) {
  v = fmaxf(oct_max(v), dpp<X8>(oct_max(v)));
  { auto r = __builtin_amdgcn_permlane16_swap(asu(v), asu(v), false, false); v = fmaxf(asf(r[0]), asf(r[1])); }
  { auto r = __builtin_amdgcn_permlane32_swap(asu(v), asu(v), false, false); v = fmaxf(asf(r[0]), asf(r[1])); }
  return v;
}
__device__ __forceinline__ void pack_rows(float v, unsigned& p01, unsigned& p23) {
  auto r = __builtin_amdgcn_permlane16_swap(asu(v), asu(v), false, false);
  unsigned p = pk(asf(r[0]), asf(r[1]));
  auto q = __builtin_amdgcn_permlane32_swap(p, p, false, false);
  p01 = q[0]; p23 = q[1];
}
__device__ __forceinline__ float quad_cols(const float (&y)[4], bool b3, bool b2) {
  float t = merge<X7>(merge<X8>(y[0], y[1], b3), merge<X8>(y[2], y[3], b3), b2);
  t += dpp<X2>(t); t += dpp<X1>(t);
  return t;
}
__device__ __forceinline__ unsigned filled(float v) { return min(asu(v), EMPTY - 1u); }
__device__ __forceinline__ rsrc_t mk(const void* p, unsigned bytes) { return __builtin_amdgcn_make_buffer_rsrc((void*)p, 0, (int)bytes, 0x00020000); }
__device__ __forceinline__ unsigned now() { return (unsigned)__builtin_amdgcn_s_memrealtime(); }
__device__ __forceinline__ void lds_barrier() { asm volatile("s_waitcnt lgkmcnt(0)\n\ts_barrier" ::: "memory"); }
__device__ __forceinline__ bool any(bool p) { return __builtin_amdgcn_ballot_w64(p) != 0; }
__device__ __forceinline__ unsigned max8(const unsigned (&v)[8]) { return max(max(max(v[0], v[1]), max(v[2], v[3])), max(max(v[4], v[5]), max(v[6], v[7]))); }
__device__ __forceinline__ float sum8(const unsigned (&v)[8]) { return ((asf(v[0]) + asf(v[1])) + (asf(v[2]) + asf(v[3]))) + ((asf(v[4]) + asf(v[5])) + (asf(v[6]) + asf(v[7]))); }
__device__ __forceinline__ unsigned max4(const u32x4& v) { return max(max(v[0], v[1]), max(v[2], v[3])); }
__device__ __forceinline__ float share_sum(const u32x4& v, bool b3) {
  const float s0 = sw32(lo16(v[0]), lo16(v[2])), s1 = sw32(hi16(v[0]), hi16(v[2])), s2 = sw32(lo16(v[1]), lo16(v[3])), s3 = sw32(hi16(v[1]), hi16(v[3]));
  return merge<X8>(sw16(s0, s2), sw16(s1, s3), b3);
}
#define FENCE() asm volatile("" ::: "memory")
#define SB() __builtin_amdgcn_sched_barrier(0)
constexpr int bitrev4(int i) { return ((i & 1) << 3) | ((i & 2) << 1) | ((i & 4) >> 1) | ((i & 8) >> 3); }
constexpr float LOG2E = 1.4426950408889634f;
constexpr unsigned ONES = 0x3f803f80u;


struct alignas(16) Lds {
  u32x2 wo[8][64][4];
  u32x2 w2[7168];
  float a[8][128];
  float y[8][16];
  float g[20][8];
  float cs[16][8];
  float o[16][8];
  float hres[16];
  u32x4 x[64];
  unsigned q[64];
  u32x2 ml[10];
  float pad[3000];
};
__device__ __forceinline__ unsigned lds_addr(const void* p) { return (unsigned)(size_t)(const __attribute__((address_space(3))) void*)p; }
__device__ __forceinline__ u32x4 srd(const void* p, unsigned bytes) { const unsigned long long a = (unsigned long long)p; return (u32x4){(unsigned)a, (unsigned)(a >> 32) & 0xffffu, bytes, 0x00020000u}; }
__device__ __forceinline__ void dma(const u32x4& d, unsigned vo, unsigned so, unsigned dst) {
  unsigned keep;
  asm volatile("s_mov_b32 %0, m0\n\ts_mov_b32 m0, %1\n\ts_nop 0\n\tbuffer_load_dwordx4 %2, %3, %4 offen lds\n\ts_mov_b32 m0, %0" : "=&s"(keep) : "s"(dst), "v"(vo), "s"(d), "s"(so) : "memory");
}
template <int I> __device__ __forceinline__ void vstep(const u32x4& v, float p, float (&acc)[8]) {
  const float pb = dpp<0x150 + bitrev4(I)>(p);
#pragma unroll
  for (int k = 0; k < 4; ++k) { acc[2 * k] = fmaf(pb, lo16(v[k]), acc[2 * k]); acc[2 * k + 1] = fmaf(pb, hi16(v[k]), acc[2 * k + 1]); }
}
template <int N> struct IC { static constexpr int v = N; };
template <int I, int N, class F> __device__ __forceinline__ void unroll(F&& f) { if constexpr (I < N) { f(IC<I>{}); unroll<I + 1, N>(f); } }
__device__ __forceinline__ u32x4 pair_rows(const u32x2& a, const u32x2& b) { return (u32x4){plo(a[0], b[0]), phi(a[0], b[0]), plo(a[1], b[1]), phi(a[1], b[1])}; }

__global__ void __launch_bounds__(640, 3) fused_decode(const unsigned short* __restrict__ xin, const unsigned short* __restrict__ Kc, const unsigned short* __restrict__ Vc,
    const unsigned short* __restrict__ wq, const unsigned short* __restrict__ wo, const unsigned short* __restrict__ wg, const unsigned short* __restrict__ wu,
    const unsigned short* __restrict__ wd, unsigned short* __restrict__ out, unsigned char* __restrict__ ws) {
  __shared__ Lds L;
  unsigned t0 = now();
  asm volatile("" : "+s"(xin), "+s"(Kc), "+s"(Vc), "+s"(wq), "+s"(wo), "+s"(wg), "+s"(wu), "+s"(wd), "+s"(out), "+s"(ws));
  const int tid = threadIdx.x, lane = tid & 63, r = lane >> 4, c = lane & 15;
  const int w = __builtin_amdgcn_readfirstlane(tid >> 6);
  const int b = blockIdx.x, h = b & 31, s = b >> 5, hg = b & 7, cg = b >> 3;
  const bool ffn1 = cg < 28;
  const int wc = cg ^ WCX;
  const int fg = hg * 28 + cg, og = hg * 4 + (cg - 28);
  const bool b3 = lane & 8, b2 = lane & 4, b1 = lane & 2, b0 = lane & 1;
  const int qcol = 4 * r + (b3 ? 1 : 0) + (b2 ? 2 : 0);
  const rsrc_t rws = mk(ws, WS_BYTES);
  int spins = 0;
  unsigned long long pc; unsigned cend;
  asm volatile("s_getpc_b64 %0\n.Lcw_0:\n\ts_mov_b32 %1, .Lcw_1-.Lcw_0\n\ts_max_i32 %1, %1, .Lcw_2-.Lcw_0\n\ts_max_i32 %1, %1, 0" : "=&s"(pc), "=&s"(cend) :: "scc");
  const u32x4 rc = srd((const void*)pc, cend);
  asm volatile("s_cmp_lg_u32 %0, 9\n\ts_cbranch_scc1 1f\n\ts_mov_b32 m0, %1\n\ts_nop 0\n\tbuffer_load_dword %2, %3, 0 offen lds\n\tbuffer_load_dword %2, %3, %4 offen lds\n\tbuffer_load_dword %2, %3, %5 offen lds\n\tbuffer_load_dword %2, %3, %6 offen lds\n1:"
                       :: "s"(w), "s"(lds_addr(L.pad)), "v"(lane * 128), "s"(rc), "s"(8192), "s"(16384), "s"(24576) : "scc", "memory");
#define WAIT_UNTIL(tq) do { while ((int)((now() << 8) - (tq)) < 0) __builtin_amdgcn_s_sleep(1); } while (0)
#define SPIN_UNTIL(tq) do { const unsigned t_ = (tq); while ((int)((now() << 8) - t_) < 0) {} } while (0)

  if (__builtin_expect(w == 8, 1)) {
    const u32x4 empty4 = {EMPTY, EMPTY, EMPTY, EMPTY};
    const unsigned pa_off = O_PA + (h * 8 + s) * 256, y_off = O_Y + hg * 1024 + wc * 32;
    __builtin_amdgcn_s_setprio(3);
    const rsrc_t rx = mk(xin, 1024), rq = mk(wq, 4u << 20);
    const u32x4 xf = __builtin_amdgcn_raw_buffer_load_b128(rx, lane * 16, 0, 0);
    const u32x4 xp0 = __builtin_amdgcn_raw_buffer_load_b128(rx, r * 32, hg * 128, 0);
    const u32x4 xp1 = __builtin_amdgcn_raw_buffer_load_b128(rx, r * 32 + 16, hg * 128, 0);
    SB();
    u32x4 wqv[16];
#pragma unroll
    for (int i = 0; i < 16; ++i) wqv[i] = __builtin_amdgcn_raw_buffer_load_b128(rq, r * (16 * 8192) + c * 16, (hg * 64 + i) * 8192 + cg * 256, NT);
    const u32x2 ew = __builtin_amdgcn_raw_buffer_load_b64(rws, O_E, 0, 0);
    SB();
    if (lane < 10) L.ml[lane] = (u32x2){EMPTY, EMPTY};
    L.q[lane] = EMPTY;
    lds_barrier();
    __builtin_amdgcn_raw_buffer_store_b128(empty4, rws, lane < 16 ? pa_off + lane * 16 : lane == 16 ? O_ML + h * LSTR + s * 16 : (lane == 24 && ffn1) ? O_ACT + fg * 16
                                                        : (lane >= 32 && lane < 34) ? y_off + (lane & 1) * 16 : OOB, 0, SC1);
    SB();
    L.x[lane] = xf;
    unsigned xn[8];
    {
      float s1 = 0.f;
#pragma unroll
      for (int k = 0; k < 4; ++k) s1 += lo16(xf[k]) + hi16(xf[k]);
      const float mu = wave_sum(s1) * (1.f / 512);
      float s2 = 0.f;
#pragma unroll
      for (int k = 0; k < 4; ++k) { const float a = lo16(xf[k]) - mu, d = hi16(xf[k]) - mu; s2 = fmaf(a, a, s2); s2 = fmaf(d, d, s2); }
      const float var = rbf(wave_sum(s2) * (1.f / 512));
      const float m = rbf(mu), rs = 1.f / rbf(sqrtf(rbf(var + 1e-5f)));
#pragma unroll
      for (int k = 0; k < 8; ++k) { const unsigned u = k < 4 ? xp0[k & 3] : xp1[k & 3]; const unsigned d = pk(lo16(u) - m, hi16(u) - m); xn[k] = pk(lo16(d) * rs, hi16(d) * rs); }
    }
    asm volatile("" :: "v"(xn[0]), "v"(xn[7]));
    float a[8];
#pragma unroll
    for (int k = 0; k < 8; ++k) a[k] = 0.f;
#pragma unroll
    for (int j = 0; j < 8; ++j)
#pragma unroll
      for (int k = 0; k < 4; ++k) {
        a[2 * k] = dot2(plo(wqv[2 * j][k], wqv[2 * j + 1][k]), xn[j], a[2 * k]);
        a[2 * k + 1] = dot2(phi(wqv[2 * j][k], wqv[2 * j + 1][k]), xn[j], a[2 * k + 1]);
      }
    const unsigned tlo = ew[0] + 1u, thi = ew[1] * 747796405u + ew[0] + 2891336453u;
    {
      const float s0 = sw32(a[0], a[4]), s1 = sw32(a[1], a[5]), s2 = sw32(a[2], a[6]), s3 = sw32(a[3], a[7]);
      __builtin_amdgcn_raw_buffer_store_b64((u32x2){asu(sw16(s0, s2)), tlo}, rws, lane * 8, O_QP + (cg * 8 + hg) * 1024, SC1);
      __builtin_amdgcn_raw_buffer_store_b64((u32x2){asu(sw16(s1, s3)), tlo}, rws, lane * 8 + 512, O_QP + (cg * 8 + hg) * 1024, SC1);
    }
    WAIT_UNTIL((t0 + QP_AT + s * QP_ST) << 8);
    {
      u32x2 v[16];
#pragma unroll 1
      for (;;) {
        FENCE();
#pragma unroll
        for (int u = 0; u < 16; ++u) v[u] = __builtin_amdgcn_raw_buffer_load_b64(rws, lane * 8 + (u & 1) * 512, O_QP + h * 8192 + (u >> 1) * 1024, SC1);
        unsigned bad = 0;
#pragma unroll
        for (int u = 0; u < 16; ++u) bad |= v[u][1] ^ tlo;
        if (!any(bad != 0) || ++spins > SPIN_MAX) break;
        __builtin_amdgcn_s_sleep(30);
      }
      unsigned e[8], o[8];
#pragma unroll
      for (int u = 0; u < 8; ++u) { e[u] = v[2 * u][0]; o[u] = v[2 * u + 1][0]; }
      L.q[4 * c + r] = min(pk(sum8(e), sum8(o)), EMPTY - 1u);
    }
    unsigned te;
    {
      WAIT_UNTIL((t0 + FLAG_AT) << 8);
      asm volatile("s_waitcnt vmcnt(0)" ::: "memory");
      __builtin_amdgcn_raw_buffer_store_b96((u32x3){tlo ^ ((unsigned)b * GOLD), thi, t0}, rws, lane == 0 ? O_FLAG + cg * LSTR + hg * 16 : OOB, 0, SC1);
      if (b == 0) {
        WAIT_UNTIL((t0 + ALL_AT) << 8);
        u32x3 f[4];
#pragma unroll 1
        for (;;) {
          FENCE();
#pragma unroll
          for (int u = 0; u < 4; ++u) f[u] = __builtin_amdgcn_raw_buffer_load_b96(rws, (lane >> 3) * LSTR + (lane & 7) * 16 + u * (8 * LSTR), O_FLAG, SC1);
          unsigned bad = 0;
#pragma unroll
          for (int u = 0; u < 4; ++u) bad |= (f[u][0] ^ tlo ^ ((unsigned)(lane + 64 * u) * GOLD)) | (f[u][1] ^ thi);
          if (!any(bad != 0) || ++spins > SPIN_MAX) break;
          __builtin_amdgcn_s_sleep(16);
        }
        FENCE();
        __builtin_amdgcn_raw_buffer_store_b64((u32x2){tlo, thi}, rws, lane == 0 ? O_E : OOB, 0, SC1);
        const int d = max(max((int)(f[0][2] - t0), (int)(f[1][2] - t0)), max((int)(f[2][2] - t0), (int)(f[3][2] - t0)));
        const unsigned last = t0 + (unsigned)__builtin_amdgcn_readfirstlane((int)wave_max((float)min(d, 300)));
        __builtin_amdgcn_raw_buffer_store_b96((u32x3){~tlo, thi + 1u, last}, rws, lane < 32 ? O_GO + lane * LSTR : OOB, 0, SC1);
      }
      WAIT_UNTIL((t0 + GO_AT) << 8);
      u32x3 g;
#pragma unroll 1
      for (;;) {
        FENCE();
        g = __builtin_amdgcn_raw_buffer_load_b96(rws, 0, O_GO + cg * LSTR, SC1);
        if ((__builtin_amdgcn_readfirstlane(g[0]) == ~tlo && __builtin_amdgcn_readfirstlane(g[1]) == thi + 1u) || ++spins > SPIN_MAX) break;
        __builtin_amdgcn_s_sleep(24);
      }
      FENCE();
      te = min(((t0 + min(max((int)(__builtin_amdgcn_readfirstlane(g[2]) - t0), 0), 300)) << 8) + ENDQ, EMPTY - 1u);
      if (lane == 0) L.ml[9][0] = te;
    }
    {
      u32x2 v;
#pragma unroll 1
      for (;;) {
        FENCE();
        v = L.ml[lane & 7];
        if (!any(max(v[0], v[1]) == EMPTY) || ++spins > SPIN_MAX) break;
        __builtin_amdgcn_s_sleep(2);
      }
      const float M = oct_max(asf(v[0]));
      const float l = oct_sum(asf(v[1]) * __builtin_amdgcn_exp2f((asf(v[0]) - M) * LOG2E));
      __builtin_amdgcn_raw_buffer_store_b64((u32x2){filled(M), filled(l)}, rws, lane == 0 ? O_ML + h * LSTR + s * 16 : OOB, 0, SC1);
    }
    float hM, hl;
    {
      u32x2 v;
      WAIT_UNTIL(te - ML_AT * 256);
#pragma unroll 1
      for (;;) {
        FENCE();
        v = __builtin_amdgcn_raw_buffer_load_b64(rws, (lane & 7) * 16, O_ML + h * LSTR, SC1);
        if (!any(max(v[0], v[1]) == EMPTY) || ++spins > SPIN_MAX) break;
        __builtin_amdgcn_s_sleep(40);
      }
      hM = oct_max(asf(v[0]));
      hl = 1.f / oct_sum(asf(v[1]) * __builtin_amdgcn_exp2f((asf(v[0]) - hM) * LOG2E));
    }
    if (lane == 0) L.ml[8] = (u32x2){filled(hM), filled(hl)};
    lds_barrier();
    {
      f32x2 v[8];
#pragma unroll
      for (int u = 0; u < 8; ++u) v[u] = *(const f32x2*)&L.a[u][2 * lane];
      const f32x2 t = ((v[0] + v[1]) + (v[2] + v[3])) + ((v[4] + v[5]) + (v[6] + v[7]));
      __builtin_amdgcn_raw_buffer_store_b32(min(pk(t[0], t[1]), EMPTY - 1u), rws, lane * 4, pa_off, SC1);
    }
    lds_barrier();
    {
      f32x2 t = *(const f32x2*)&L.y[lane >> 3][2 * (lane & 7)];
      t[0] += dpp<X8>(t[0]); t[1] += dpp<X8>(t[1]);
      t[0] = sw16(t[0], t[0]); t[1] = sw16(t[1], t[1]);
      t[0] = sw32(t[0], t[0]); t[1] = sw32(t[1], t[1]);
      const unsigned py = min(pk(t[0], t[1]), EMPTY - 1u);
      __builtin_amdgcn_raw_buffer_store_b32(py, rws, lane < 8 ? lane * 4 : OOB, y_off, SC1);
    }
    if (ffn1) {
      const float csg = oct_sum(L.cs[lane >> 3][lane & 7]), csu = oct_sum(L.cs[8 + (lane >> 3)][lane & 7]);
      asm volatile("" :: "v"(csg), "v"(csu));
      lds_barrier();
      float g = L.g[lane >> 3][lane & 7], u = L.g[8 + (lane >> 3)][lane & 7], s1 = L.g[16][lane & 7], s2 = L.g[17][lane & 7];
      s1 += dpp<X1>(s1); s2 += dpp<X1>(s2); g += dpp<X1>(g); u += dpp<X1>(u);
      s1 += dpp<X2>(s1); s2 += dpp<X2>(s2); g += dpp<X2>(g); u += dpp<X2>(u);
      s1 += dpp<X7>(s1); s2 += dpp<X7>(s2); g += dpp<X7>(g); u += dpp<X7>(u);
      const float mu = s1 * (1.f / 512);
      const float var = rbf(fmaxf(fmaf(-mu, mu, s2 * (1.f / 512)), 0.f));
      const float m = rbf(mu), rs = __builtin_amdgcn_rcpf(rbf(__builtin_amdgcn_sqrtf(rbf(var + 1e-5f))));
      g = rbf(fmaf(-m, csg, g) * rs); u = rbf(fmaf(-m, csu, u) * rs);
      const float sg = rbf(g * __builtin_amdgcn_rcpf(1.f + __builtin_amdgcn_exp2f(-g * LOG2E)));
      const float a = sg * u;
      const unsigned pa = min(pk(a, dpp<X8>(a)), EMPTY - 1u);
      __builtin_amdgcn_raw_buffer_store_b32(pa, rws, c == 0 ? r * 4 : OOB, O_ACT + fg * 16, SC1);
      asm volatile(".Lcw_1:");
    } else {
      lds_barrier();
      const f32x2 hh = *(const f32x2*)&L.hres[2 * (lane >> 3)];
      const float h0 = rbf(hh[0]), h1 = rbf(hh[1]);
      lds_barrier();
      const float d0 = oct_sum(L.o[2 * (lane >> 3)][lane & 7]), d1 = oct_sum(L.o[2 * (lane >> 3) + 1][lane & 7]);
      unsigned po = pk(h0 + rbf(d0), h1 + rbf(d1));
      if (spins > SPIN_MAX) po = 0x7fc07fc0u;
      __builtin_amdgcn_raw_buffer_store_b32(po, mk(out, 1024), (lane & 7) == 0 ? og * 32 + (lane >> 3) * 4 : OOB, 0, 0);
    }
  } else if (w < 8) {
    unsigned dtq = ffn1 ? (int)(TOTQ * SLOW / ((66 - FAST) * SLOW + FAST - 1)) : (int)(TOTQ * SLOW / ((50 - FAST) * SLOW + FAST - 1));
    unsigned tq = (t0 << 8) + OFFQ + w * (dtq >> 3);
#define PACE() do { WAIT_UNTIL(tq); tq += dtq; } while (0)
    const unsigned kvbase = (unsigned)h * (16384u * 256u) + s * (66u * 8192u) + w * 1024u;
    const rsrc_t rk = mk(Kc, 128u << 20), rv = mk(Vc, 128u << 20);
    const unsigned voff = lane * 16;
    u32x4 mk_[2], mv_[2], ext[16], buf[16];
    unroll<0, 36>([&](auto D) {
      constexpr int d = D.v;
      if (d % 2 == 0) PACE();
      if (d < 2) mk_[d & 1] = __builtin_amdgcn_raw_buffer_load_b128(rk, voff, kvbase + d * 8192, NT);
      else if (d < 4) mv_[d & 1] = __builtin_amdgcn_raw_buffer_load_b128(rv, voff, kvbase + (d - 2) * 8192, NT);
      else (d < 20 ? ext[(d - 4) & 15] : buf[(d - 4) & 15]) = __builtin_amdgcn_raw_buffer_load_b128(rk, voff, kvbase + (d - 2) * 8192, NT);
      SB();
      if (d == 1) lds_barrier();
    });
    u32x4 q;
#pragma unroll 1
    for (;;) {
      FENCE();
      q = *(const u32x4*)&L.q[4 * c];
      if (!any(max(max(q[0], q[1]), max(q[2], q[3])) == EMPTY) || ++spins > SPIN_MAX) break;
      __builtin_amdgcn_s_sleep(1);
    }
    unsigned te = (t0 << 8) + ENDQ;
    int rem = ffn1 ? 48 : 32;
    const unsigned lead = (7 - w) * (dtq >> 3);
    auto plan = [&]() {
      const unsigned e = __builtin_amdgcn_readfirstlane(L.ml[9][0]);
      if (e != EMPTY) te = e;
      { const unsigned n = now() << 8; if ((int)(n - tq) > 0) tq = n; }
      const int left = max((int)(te - lead - tq), 0);
      const float inv = __builtin_amdgcn_rcpf(rem > FAST ? (float)(rem - FAST) + (FAST - 1) / SLOW : (float)(rem - 1));
      dtq = (unsigned)__builtin_amdgcn_readfirstlane((int)((float)left * inv));
      rem -= 8;
    };
    float tp, l1, l2, l3, res;
    auto kdot = [&](const u32x4& k) { float t = dot2(k[0], q[0], 0.f); t = dot2(k[1], q[1], t); t = dot2(k[2], q[2], t); t = dot2(k[3], q[3], t); asm volatile("" : "+v"(t)); SB(); return t; };
    auto kmerge = [&](auto D, float t) {
      constexpr int d = D.v;
      if (d % 2 == 0) tp = t;
      else {
        t = merge<X8>(tp, t, b3);
        if (d % 4 == 1) l1 = t;
        else {
          t = merge<X7>(l1, t, b2);
          if (d % 8 == 3) l2 = t;
          else {
            t = merge<X2>(l2, t, b1);
            if (d == 7) l3 = t; else res = merge<X1>(l3, t, b0);
          }
        }
      }
      SB();
    };
    auto score = [](float v) { return rbf(rbf(v) * 0.08838834764831845f); };
    float scm;
    { const float ta = kdot(mk_[0]), tc = kdot(mk_[1]); scm = score(oct_sum(merge<X8>(ta, tc, b3))); }
    unroll<0, 16>([&](auto D) { kmerge(D, kdot(ext[D.v])); });
    float sc0 = -3.0e38f, sc1 = -3.0e38f, sc2 = -3.0e38f, sc3 = score(res);
    unsigned ko = kvbase + 34u * 8192u;
#pragma unroll 1
    for (int it = ffn1 ? 1 : 2; it < 4; ++it) {
      const rsrc_t rn = mk(it == 3 ? Vc : Kc, 128u << 20);
      const unsigned so = it == 3 ? kvbase + 2u * 8192u : ko;
      ko += 16u * 8192u;
      plan();
      unroll<0, 16>([&](auto D) {
        constexpr int d = D.v;
        const float t = kdot(buf[d]);
        if (d % 2 == 0) PACE();
        buf[d] = __builtin_amdgcn_raw_buffer_load_b128(rn, voff, so + d * 8192, NT);
        kmerge(D, t);
      });
      sc0 = sc1; sc1 = sc2; sc2 = sc3;
      sc3 = score(res);
    }
    const float mw = fmaxf(wave_max(fmaxf(fmaxf(fmaxf(sc0, sc1), fmaxf(sc2, sc3)), scm)), -3.0e38f);
    const float p0 = ffn1 ? __builtin_amdgcn_exp2f((sc0 - mw) * LOG2E) : 0.f, p1 = __builtin_amdgcn_exp2f((sc1 - mw) * LOG2E);
    const float p2 = __builtin_amdgcn_exp2f((sc2 - mw) * LOG2E), p3 = __builtin_amdgcn_exp2f((sc3 - mw) * LOG2E);
    const float pm = __builtin_amdgcn_exp2f((scm - mw) * LOG2E);
    {
      const float lw = wave_sum((p0 + p1) + (p2 + p3) + ((lane & 7) == 0 ? pm : 0.f));
      if (lane == 0) L.ml[w] = (u32x2){filled(mw), filled(lw)};
    }
    SB();
    float acc[8];
#pragma unroll
    for (int k = 0; k < 8; ++k) acc[k] = 0.f;
#define PIN_ACC() asm volatile("" : "+v"(acc[0]), "+v"(acc[1]), "+v"(acc[2]), "+v"(acc[3]), "+v"(acc[4]), "+v"(acc[5]), "+v"(acc[6]), "+v"(acc[7]))
    vstep<0>(mv_[0], pm, acc); vstep<1>(mv_[1], pm, acc);
    PIN_ACC(); SB();
    float pc = ffn1 ? p0 : p1;
    ko = kvbase + 18u * 8192u;
#pragma unroll 1
    for (int it = ffn1 ? 0 : 1; it < 2; ++it) {
      plan();
      unroll<0, 16>([&](auto D) {
        constexpr int d = D.v;
        vstep<d>(buf[d], pc, acc);
        PIN_ACC(); SB();
        if (d % 2 == 0) PACE();
        buf[d] = __builtin_amdgcn_raw_buffer_load_b128(rv, voff, ko + d * 8192, NT);
        SB();
      });
      pc = p1; ko += 16u * 8192u;
    }
    float cw, xo;
    u32x4 wo0, wo1, wv[7];
    plan();
    unroll<0, 16>([&](auto D) {
      constexpr int d = D.v;
      vstep<d>(buf[d], p2, acc);
      PIN_ACC(); SB();
      if (d % 2 == 0) PACE();
      buf[d] = __builtin_amdgcn_raw_buffer_load_b128(rv, voff, ko + d * 8192, NT);
      SB();
      if constexpr (d % 2 == 1 && d > 2 * CATCH) { vstep<d - 2 * CATCH - 1>(buf[d - 2 * CATCH - 1], p3, acc); PIN_ACC(); SB(); vstep<d - 2 * CATCH>(buf[d - 2 * CATCH], p3, acc); PIN_ACC(); SB(); }
      if constexpr (d == 2 * PRE + 1) {
        u32x2 v;
#pragma unroll 1
        for (;;) {
          FENCE();
          v = L.ml[8];
          if (!any(max(max(v[0], v[1]), L.ml[9][1]) == EMPTY) || ++spins > SPIN_MAX) break;
        }
        cw = __builtin_amdgcn_exp2f((mw - asf(v[0])) * LOG2E) * asf(v[1]);
        xo = asf((unsigned)((const unsigned short*)L.x)[w * 64 + 8 * (c & 7) + 2 * r + (c >> 3)] << 16);
        wo0 = pair_rows(L.wo[w][c][r], L.wo[w][16 + c][r]);
        wo1 = pair_rows(L.wo[w][32 + c][r], L.wo[w][48 + c][r]);
        if (ffn1) {
          const u32x2* g = &L.w2[(r >> 1) * 1024 + (w * 64 + c) * 2 + (r & 1)];
          wv[0] = pair_rows(g[0], g[32]); wv[1] = pair_rows(g[64], g[96]);
          float y[4];
#pragma unroll
          for (int n = 0; n < 4; ++n) y[n] = dot2(wv[0][n], ONES, dot2(wv[1][n], ONES, 0.f));
          const float t = quad_cols(y, b3, b2);
          if ((lane & 3) == 0) L.cs[qcol][w] = t;
#pragma unroll
          for (int j = 2; j < 7; ++j) wv[j] = wv[0];
        } else {
#pragma unroll
          for (int j = 0; j < 7; ++j) wv[j] = pair_rows(L.w2[(w * 7 + j) * 128 + c * 4 + r], L.w2[(w * 7 + j) * 128 + 64 + c * 4 + r]);
        }
        asm volatile("" : "+v"(wo0), "+v"(wo1), "+v"(cw), "+v"(xo), "+v"(wv[0]), "+v"(wv[1]), "+v"(wv[2]), "+v"(wv[3]), "+v"(wv[4]), "+v"(wv[5]), "+v"(wv[6]));
        SB();
      }
    });
    unroll<16 - 2 * CATCH, 16>([&](auto D) {
      constexpr int d = D.v;
      vstep<d>(buf[d], p3, acc);
      PIN_ACC(); SB();
    });
    {
      const float s0 = sw32(acc[0], acc[4]), s1 = sw32(acc[1], acc[5]), s2 = sw32(acc[2], acc[6]), s3 = sw32(acc[3], acc[7]);
      *(f32x2*)&L.a[w][8 * c + 2 * r] = (f32x2){sw16(s0, s2) * cw, sw16(s1, s3) * cw};
    }
    lds_barrier();
    te = __builtin_amdgcn_readfirstlane(L.ml[9][0]);
    {
      u32x4 v;
      const unsigned so = O_PA + (hg + 8 * (w >> 1)) * 2048 + (w & 1) * 128;
      SPIN_UNTIL((now() + E1) << 8); SPIN_UNTIL(te + D1 * 256);
#pragma unroll 1
      for (;;) {
        FENCE();
        v = __builtin_amdgcn_raw_buffer_load_b128(rws, (lane >> 3) * 256 + (lane & 7) * 16, so, SC1);
        if (!any(max4(v) == EMPTY) || ++spins > SPIN_MAX) break;
      }
      unsigned p01, p23; pack_rows(share_sum(v, b3), p01, p23);
      float y[4];
#pragma unroll
      for (int n = 0; n < 4; ++n) y[n] = dot2(wo0[n], p01, dot2(wo1[n], p23, 0.f));
      const float t = quad_cols(y, b3, b2);
      if ((lane & 3) == 0) L.y[w][qcol] = t;
    }
    lds_barrier();
    float hv;
    {
      u32x4 v;
      SPIN_UNTIL((now() + E2) << 8); SPIN_UNTIL(te + (D1 + D2) * 256);
#pragma unroll 1
      for (;;) {
        FENCE();
        v = __builtin_amdgcn_raw_buffer_load_b128(rws, (lane >> 3) * 1024 + (lane & 7) * 16, O_Y + w * 128, SC1);
        if (!any(max4(v) == EMPTY) || ++spins > SPIN_MAX) break;
      }
      hv = xo + rbf(share_sum(v, b3));
    }
    if (ffn1) {
      unsigned p01, p23; pack_rows(hv, p01, p23);
      float y[4];
#pragma unroll
      for (int n = 0; n < 4; ++n) y[n] = dot2(wv[0][n], p01, dot2(wv[1][n], p23, 0.f));
      float z = dot2(p01, (r & 1) ? p01 : ONES, dot2(p23, (r & 1) ? p23 : ONES, 0.f));
      const float t = quad_cols(y, b3, b2);
      z = row_sum(z);
      if ((lane & 3) == 0) L.g[qcol][w] = t;
      if (c == 1) L.g[16 + r][w] = z;
      lds_barrier();
      asm volatile(".Lcw_2:");
    } else {
      u32x4 va; u32x3 vb;
      if (w * 4 + ((c & 7) >> 1) == og) L.hres[8 * (c & 1) + 2 * r + (c >> 3)] = hv;
      lds_barrier();
      SPIN_UNTIL((now() + E3) << 8); SPIN_UNTIL(te + (D1 + D2 + D3) * 256);
#pragma unroll 1
      for (;;) {
        FENCE();
        va = __builtin_amdgcn_raw_buffer_load_b128(rws, c * 28, O_ACT + w * 448, SC1);
        vb = __builtin_amdgcn_raw_buffer_load_b96(rws, c * 28 + 16, O_ACT + w * 448, SC1);
        if (!any(max(max(max(va[0], va[1]), max(va[2], va[3])), max(max(vb[0], vb[1]), vb[2])) == EMPTY) || ++spins > SPIN_MAX) break;
      }
      float y[4];
#pragma unroll
      for (int n = 0; n < 4; ++n) {
        y[n] = dot2(wv[0][n], va[0], 0.f); y[n] = dot2(wv[1][n], va[1], y[n]); y[n] = dot2(wv[2][n], va[2], y[n]); y[n] = dot2(wv[3][n], va[3], y[n]);
        y[n] = dot2(wv[4][n], vb[0], y[n]); y[n] = dot2(wv[5][n], vb[1], y[n]); y[n] = dot2(wv[6][n], vb[2], y[n]);
      }
      const float t = quad_cols(y, b3, b2);
      if ((lane & 3) == 0) L.o[qcol][w] = t;
      lds_barrier();
    }
  } else {
    lds_barrier();
    const u32x4 ro = srd(wo, 4u << 20), r1 = ffn1 ? srd(wg, 512 * 3584) : srd(wd, 1792 * 1024), r2 = srd(wu, 512 * 3584);
    const unsigned vo = (8 * ((lane >> 1) & 7) + ((lane >> 4) & 1) + 2 * (lane >> 5)) * 1024 + (lane & 1) * 16;
    const unsigned vg = (8 * (lane & 7) + ((lane >> 3) & 1) + 2 * (lane >> 4)) * 3584;
    const unsigned vd = ((lane >> 1) & 15) * (14 * 1024) + (lane >> 5) * 1024 + (lane & 1) * 16;
    WAIT_UNTIL((t0 << 8) + WPRE);
    {
      const u32x4 rd = srd(wd, 1792 * 1024);
      unsigned keep;
      asm volatile("s_mov_b32 %0, m0\n\ts_mov_b32 m0, %1\n\ts_nop 0\n\tbuffer_load_dword %2, %3, 0 offen lds\n\ts_mov_b32 m0, %0"
                   : "=&s"(keep) : "s"(lds_addr(L.pad)), "v"((cg * 64 + lane) * 1024 + hg * 128), "s"(rd) : "memory");
    }
    unsigned th = (t0 << 8) + W0;
#pragma unroll 1
    for (int n = 0; n < (ffn1 ? 32 : 72); ++n) {
      const unsigned dth = !ffn1 ? (W1 - W0) / 71 : n < 16 ? (WM - W0) / 16 : (WE - WM) / 16;
      WAIT_UNTIL(th); th += dth;
      if (n < 16) {
        const int t = n ^ (cg & 3);
        dma(ro, vo, ((hg + 8 * (t >> 2)) * 128 + (t & 2) * 32 + (t & 1) * 4) * 1024 + wc * 32, lds_addr(L.wo) + t * 1024);
      } else if (ffn1) {
        const int g = n - 16;
#pragma unroll 1
        for (int p = 0; p < 2; ++p) {
          if (p) WAIT_UNTIL(th - dth / 2);
          if (lane >> 5 == p) dma((g & 8) ? r2 : r1, vg, (g & 7) * (64 * 3584) + fg * 16, lds_addr(L.w2) + g * 1024);
        }
      } else {
        const int d = (n - 16) ^ (og & 3);
        dma(r1, vd, (d / 7 * 224 + d % 7 * 2) * 1024 + og * 32, lds_addr(L.w2) + d * 1024);
      }
    }
    asm volatile("s_waitcnt vmcnt(0)" ::: "memory");
    if (lane == 0) L.ml[9][1] = 0u;
  }
}
}

extern "C" void kernel_launch(void* const* d_in, const int* in_sizes, int n_in, void* d_out, int out_size, void* d_ws, size_t ws_size, hipStream_t stream) {
  fused_decode<<<256, 640, 0, stream>>>((const unsigned short*)d_in[0], (const unsigned short*)d_in[1], (const unsigned short*)d_in[2], (const unsigned short*)d_in[3],
      (const unsigned short*)d_in[4], (const unsigned short*)d_in[6], (const unsigned short*)d_in[7], (const unsigned short*)d_in[8], (unsigned short*)d_out, (unsigned char*)d_ws);
}
```
